```python
import torch

def _softmax(x, axis=-1):
    return torch.softmax(x, dim=axis)
def _gelu(x):
    return torch.nn.functional.gelu(x, approximate="tanh")
def _silu(x):
    return torch.nn.functional.silu(x)
def _sigmoid(x):
    return torch.sigmoid(x)
def _layernorm(x, axis=-1, eps=1e-5):
    m = x.mean(dim=axis, keepdim=True)
    v = x.var(dim=axis, keepdim=True, unbiased=False)
    return (x - m) / torch.sqrt(v + eps)
def _scatter_add(tgt, idx, src, axis=0):
    return tgt.clone().index_add_(axis, idx, src)
def _segment_sum(src, seg, n):
    out = torch.zeros((n,) + src.shape[1:], dtype=src.dtype)
    return out.index_add_(0, seg, src)
def _pad(x, pw):
    flat = tuple(v for pair in reversed(pw) for v in pair)
    return torch.nn.functional.pad(x, flat)

kernel_name = 'standalone__rezero__sampling'

def setup_inputs():

    _w = (lambda a: (lambda t: t.to(torch.bfloat16) if t.is_floating_point() else t)(torch.as_tensor(a)))
    B, S, D = 1024, 16, 448
    return {
        "x": _w((lambda *s: torch.randn(*s, dtype=torch.bfloat16) * 0.05)(B, S, D)),
        "fx": _w((lambda *s: torch.randn(*s, dtype=torch.bfloat16) * 0.8)(B, S, D)),
        "alpha": _w((lambda *s: torch.randn(*s, dtype=torch.bfloat16) * 0.8)(1) * 0.1),
    }

def reference(x, fx, alpha):
    return x + alpha * fx
```

```cpp
#define BLOCK 256
#define UNROLL 7
#include <hip/hip_runtime.h>
#include <hip/hip_bf16.h>
#include <stdint.h>

typedef __attribute__((ext_vector_type(4))) unsigned int u32x4;
typedef __attribute__((ext_vector_type(2))) float f32x2;
typedef __attribute__((ext_vector_type(2))) __bf16 bf16x2;

__device__ __forceinline__ unsigned rz_pair(unsigned xw, unsigned fw, float a) {
    f32x2 f = {__uint_as_float(fw << 16), __uint_as_float(fw & 0xffff0000u)};
    f32x2 t = f * a;
    unsigned tw = __builtin_bit_cast(unsigned, __builtin_convertvector(t, bf16x2));
    f32x2 tf = {__uint_as_float(tw << 16), __uint_as_float(tw & 0xffff0000u)};
    f32x2 xf = {__uint_as_float(xw << 16), __uint_as_float(xw & 0xffff0000u)};
    f32x2 o = xf + tf;
    return __builtin_bit_cast(unsigned, __builtin_convertvector(o, bf16x2));
}

__global__ __launch_bounds__(BLOCK) void rezero_kernel(const u32x4* __restrict__ x,
                                                       const u32x4* __restrict__ fx,
                                                       const unsigned short* __restrict__ alpha,
                                                       u32x4* __restrict__ out, int nvec) {
    const int base = blockIdx.x * (BLOCK * UNROLL) + threadIdx.x;
    u32x4 xv[UNROLL], fv[UNROLL];
    if (base - (int)threadIdx.x + BLOCK * UNROLL <= nvec) {
#pragma unroll
        for (int k = 0; k < UNROLL; ++k) {
            xv[k] = x[base + k * BLOCK];
            fv[k] = fx[base + k * BLOCK];
        }
        const float a = __uint_as_float((unsigned)alpha[0] << 16);
#pragma unroll
        for (int k = 0; k < UNROLL; ++k) {
            u32x4 o;
#pragma unroll
            for (int j = 0; j < 4; ++j) o[j] = rz_pair(xv[k][j], fv[k][j], a);
            out[base + k * BLOCK] = o;
        }
    } else {
        const float a = __uint_as_float((unsigned)alpha[0] << 16);
        for (int k = 0; k < UNROLL; ++k) {
            int i = base + k * BLOCK;
            if (i < nvec) {
                u32x4 o;
                u32x4 xx = x[i], ff = fx[i];
#pragma unroll
                for (int j = 0; j < 4; ++j) o[j] = rz_pair(xx[j], ff[j], a);
                out[i] = o;
            }
        }
    }
}

__global__ void rezero_tail(const unsigned short* __restrict__ x, const unsigned short* __restrict__ fx,
                            const unsigned short* __restrict__ alpha, unsigned short* __restrict__ out,
                            int start, int n) {
    int i = start + threadIdx.x;
    if (i < n) {
        const float a = __uint_as_float((unsigned)alpha[0] << 16);
        out[i] = (unsigned short)rz_pair(x[i], fx[i], a);
    }
}

extern "C" void kernel_launch(void* const* d_in, const int* in_sizes, int n_in, void* d_out, int out_size,
                              void* d_ws, size_t ws_size, hipStream_t stream) {
    const int n = out_size;
    const int nvec = n / 8;
    if (nvec > 0) {
        const int grid = (nvec + BLOCK * UNROLL - 1) / (BLOCK * UNROLL);
        rezero_kernel<<<grid, BLOCK, 0, stream>>>((const u32x4*)d_in[0], (const u32x4*)d_in[1],
                                                   (const unsigned short*)d_in[2], (u32x4*)d_out, nvec);
    }
    if (n % 8) {
        rezero_tail<<<1, 64, 0, stream>>>((const unsigned short*)d_in[0], (const unsigned short*)d_in[1],
                                          (const unsigned short*)d_in[2], (unsigned short*)d_out, nvec * 8, n);
    }
}
```

```cpp
#define LD_AUX 0
#define ST_AUX 2
#include <hip/hip_runtime.h>
#include <hip/hip_bf16.h>
#include <stdint.h>

typedef __attribute__((ext_vector_type(4))) unsigned int u32x4;
typedef __attribute__((ext_vector_type(2))) float f32x2;
typedef __attribute__((ext_vector_type(2))) __bf16 bf16x2;

#define BLOCK 256
#define UNROLL 7

__device__ __forceinline__ unsigned rz_pair(unsigned xw, unsigned fw, float a) {
    f32x2 f = {__uint_as_float(fw << 16), __uint_as_float(fw & 0xffff0000u)};
    f32x2 t = f * a;
    unsigned tw = __builtin_bit_cast(unsigned, __builtin_convertvector(t, bf16x2));
    f32x2 tf = {__uint_as_float(tw << 16), __uint_as_float(tw & 0xffff0000u)};
    f32x2 xf = {__uint_as_float(xw << 16), __uint_as_float(xw & 0xffff0000u)};
    f32x2 o = xf + tf;
    return __builtin_bit_cast(unsigned, __builtin_convertvector(o, bf16x2));
}

__global__ __launch_bounds__(BLOCK) void rezero_kernel(const void* __restrict__ x, const void* __restrict__ fx,
                                                       const unsigned short* __restrict__ alpha,
                                                       void* __restrict__ out, unsigned nbytes) {
    __amdgpu_buffer_rsrc_t rx = __builtin_amdgcn_make_buffer_rsrc((void*)x, 0, nbytes, 0x00020000);
    __amdgpu_buffer_rsrc_t rf = __builtin_amdgcn_make_buffer_rsrc((void*)fx, 0, nbytes, 0x00020000);
    __amdgpu_buffer_rsrc_t ro = __builtin_amdgcn_make_buffer_rsrc(out, 0, nbytes, 0x00020000);
    const unsigned voff = (blockIdx.x * (BLOCK * UNROLL) + threadIdx.x) * 16u;
    u32x4 xv[UNROLL], fv[UNROLL];
#pragma unroll
    for (int k = 0; k < UNROLL; ++k) {
        fv[k] = __builtin_amdgcn_raw_buffer_load_b128(rf, voff, k * BLOCK * 16, LD_AUX);
        xv[k] = __builtin_amdgcn_raw_buffer_load_b128(rx, voff, k * BLOCK * 16, LD_AUX);
    }
    const float a = __uint_as_float((unsigned)alpha[0] << 16);
#pragma unroll
    for (int k = 0; k < UNROLL; ++k) {
        u32x4 o;
#pragma unroll
        for (int j = 0; j < 4; ++j) o[j] = rz_pair(xv[k][j], fv[k][j], a);
        __builtin_amdgcn_raw_buffer_store_b128(o, ro, voff, k * BLOCK * 16, ST_AUX);
    }
}

__global__ void rezero_tail(const unsigned short* __restrict__ x, const unsigned short* __restrict__ fx,
                            const unsigned short* __restrict__ alpha, unsigned short* __restrict__ out,
                            int start, int n) {
    int i = start + threadIdx.x;
    if (i < n) {
        const float a = __uint_as_float((unsigned)alpha[0] << 16);
        out[i] = (unsigned short)rz_pair(x[i], fx[i], a);
    }
}

extern "C" void kernel_launch(void* const* d_in, const int* in_sizes, int n_in, void* d_out, int out_size,
                              void* d_ws, size_t ws_size, hipStream_t stream) {
    const int n = out_size;
    const int nvec = n / 8;
    if (nvec > 0) {
        const int grid = (nvec + BLOCK * UNROLL - 1) / (BLOCK * UNROLL);
        rezero_kernel<<<grid, BLOCK, 0, stream>>>(d_in[0], d_in[1], (const unsigned short*)d_in[2], d_out,
                                                   (unsigned)nvec * 16u);
    }
    if (n % 8) {
        rezero_tail<<<1, 64, 0, stream>>>((const unsigned short*)d_in[0], (const unsigned short*)d_in[1],
                                          (const unsigned short*)d_in[2], (unsigned short*)d_out, nvec * 8, n);
    }
}
```

```cpp
#include <hip/hip_runtime.h>
#include <stdint.h>

typedef __attribute__((ext_vector_type(2))) float f32x2;
typedef __attribute__((ext_vector_type(2))) __bf16 bf16x2;

constexpr int kBlock = 512;
constexpr int kRounds = 7;
constexpr long long kTileElems = (long long)kBlock * kRounds * 8;

#define RZ_UNPACK(LO, HI, SRC)                      \
    "v_lshlrev_b32 v" #LO ", 16, v" #SRC "\n\t"     \
    "v_and_b32 v" #HI ", s41, v" #SRC "\n\t"
#define RZ_MUL(LO, HI) "v_pk_mul_f32 v[" #LO ":" #HI "], s[42:43], v[" #LO ":" #HI "] op_sel_hi:[0,1]\n\t"
#define RZ_ADD(LO, HI, LO2, HI2) "v_pk_add_f32 v[" #LO ":" #HI "], v[" #LO2 ":" #HI2 "], v[" #LO ":" #HI "]\n\t"
#define RZ_PACK(DST, LO, HI) "v_cvt_pk_bf16_f32 v" #DST ", v" #LO ", v" #HI "\n\t"
#define RZ_CHUNK(F0, F1, F2, F3, X0, X1, X2, X3)                                                   \
    RZ_UNPACK(60, 61, F0) RZ_UNPACK(62, 63, F1) RZ_UNPACK(64, 65, F2) RZ_UNPACK(66, 67, F3)        \
    RZ_MUL(60, 61) RZ_UNPACK(68, 69, X0) RZ_MUL(62, 63) RZ_UNPACK(70, 71, X1)                      \
    RZ_MUL(64, 65) RZ_UNPACK(72, 73, X2) RZ_MUL(66, 67) RZ_UNPACK(74, 75, X3)                      \
    RZ_PACK(F0, 60, 61) RZ_PACK(F1, 62, 63) RZ_PACK(F2, 64, 65) RZ_PACK(F3, 66, 67)                \
    RZ_UNPACK(60, 61, F0) RZ_UNPACK(62, 63, F1) RZ_UNPACK(64, 65, F2) RZ_UNPACK(66, 67, F3)        \
    RZ_ADD(60, 61, 68, 69) RZ_ADD(62, 63, 70, 71) RZ_ADD(64, 65, 72, 73) RZ_ADD(66, 67, 74, 75)    \
    RZ_PACK(X0, 60, 61) RZ_PACK(X1, 62, 63) RZ_PACK(X2, 64, 65) RZ_PACK(X3, 66, 67)
#define RZ_LOAD(ADDR, F0, F3, X0, X3)                                          \
    "global_load_dwordx4 v[" #F0 ":" #F3 "], " ADDR ", %[fx] nt\n\t"           \
    "global_load_dwordx4 v[" #X0 ":" #X3 "], " ADDR ", %[x] nt\n\t"
#define RZ_STORE(ADDR, X0, X3) "global_store_dwordx4 " ADDR ", v[" #X0 ":" #X3 "], %[out] nt\n\t"
#define RZ_WAIT(N) "s_waitcnt vmcnt(" #N ")\n\t"

__global__ __launch_bounds__(kBlock) __attribute__((aligned(4096)))
void rezero_main(const void* __restrict__ x, const void* __restrict__ fx, const void* __restrict__ alpha,
                 void* __restrict__ out) {
    const unsigned voff = blockIdx.x * (unsigned)(kBlock * kRounds * 16) + threadIdx.x * 16u;
    asm volatile(
        "s_load_dword s40, %[al], 0x0\n\t"
        RZ_LOAD("%[vo]", 4, 7, 8, 11)
        "v_add_u32 v77, 0x2000, %[vo]\n\t" RZ_LOAD("v77", 12, 15, 16, 19)
        "v_add_u32 v78, 0x4000, %[vo]\n\t" RZ_LOAD("v78", 20, 23, 24, 27)
        "v_add_u32 v79, 0x6000, %[vo]\n\t" RZ_LOAD("v79", 28, 31, 32, 35)
        "v_add_u32 v80, 0x8000, %[vo]\n\t" RZ_LOAD("v80", 36, 39, 40, 43)
        "v_add_u32 v81, 0xa000, %[vo]\n\t" RZ_LOAD("v81", 44, 47, 48, 51)
        "v_add_u32 v82, 0xc000, %[vo]\n\t" RZ_LOAD("v82", 52, 55, 56, 59)
        "s_mov_b32 s41, 0xffff0000\n\t"
        "s_waitcnt lgkmcnt(0)\n\t"
        "s_lshl_b32 s42, s40, 16\n\t"
        "s_mov_b32 s43, s42\n\t"
        RZ_WAIT(12) RZ_CHUNK(4, 5, 6, 7, 8, 9, 10, 11)
        RZ_WAIT(10) RZ_CHUNK(12, 13, 14, 15, 16, 17, 18, 19)
        RZ_WAIT(8)  RZ_CHUNK(20, 21, 22, 23, 24, 25, 26, 27)
        RZ_WAIT(6)  RZ_CHUNK(28, 29, 30, 31, 32, 33, 34, 35)
        RZ_WAIT(4)  RZ_CHUNK(36, 37, 38, 39, 40, 41, 42, 43)
        "s_barrier\n\t"
        RZ_STORE("%[vo]", 8, 11)
        RZ_STORE("v77", 16, 19)
        RZ_STORE("v78", 24, 27)
        RZ_STORE("v79", 32, 35)
        RZ_STORE("v80", 40, 43)
        RZ_WAIT(7)  RZ_CHUNK(44, 45, 46, 47, 48, 49, 50, 51)
        RZ_STORE("v81", 48, 51)
        RZ_WAIT(6)  RZ_CHUNK(52, 53, 54, 55, 56, 57, 58, 59)
        RZ_STORE("v82", 56, 59)
        :
        : [x] "s"(x), [fx] "s"(fx), [al] "s"(alpha), [out] "s"(out), [vo] "v"(voff)
        : "v4", "v5", "v6", "v7", "v8", "v9", "v10", "v11", "v12", "v13", "v14", "v15", "v16", "v17", "v18", "v19",
          "v20", "v21", "v22", "v23", "v24", "v25", "v26", "v27", "v28", "v29", "v30", "v31", "v32", "v33", "v34",
          "v35", "v36", "v37", "v38", "v39", "v40", "v41", "v42", "v43", "v44", "v45", "v46", "v47", "v48", "v49",
          "v50", "v51", "v52", "v53", "v54", "v55", "v56", "v57", "v58", "v59", "v60", "v61", "v62", "v63", "v64",
          "v65", "v66", "v67", "v68", "v69", "v70", "v71", "v72", "v73", "v74", "v75", "v77", "v78", "v79", "v80",
          "v81", "v82", "s40", "s41", "s42", "s43", "scc", "memory");
}

__global__ __launch_bounds__(256) void rezero_tail(const unsigned short* __restrict__ x,
                                                   const unsigned short* __restrict__ fx,
                                                   const unsigned short* __restrict__ alpha,
                                                   unsigned short* __restrict__ out, long long start, long long n) {
    const long long i = start + (long long)blockIdx.x * 256 + threadIdx.x;
    if (i < n) {
        const float a = __uint_as_float((unsigned)alpha[0] << 16);
        const f32x2 p = {__uint_as_float((unsigned)fx[i] << 16) * a, 0.0f};
        const unsigned t = __builtin_bit_cast(unsigned, __builtin_convertvector(p, bf16x2)) << 16;
        const f32x2 s = {__uint_as_float((unsigned)x[i] << 16) + __uint_as_float(t), 0.0f};
        out[i] = (unsigned short)__builtin_bit_cast(unsigned, __builtin_convertvector(s, bf16x2));
    }
}

extern "C" void kernel_launch(void* const* d_in, const int* in_sizes, int n_in, void* d_out, int out_size,
                              void* d_ws, size_t ws_size, hipStream_t stream) {
    (void)in_sizes; (void)n_in; (void)d_ws; (void)ws_size;
    const long long n = out_size;
    const bool aligned = ((((uintptr_t)d_in[0] | (uintptr_t)d_in[1] | (uintptr_t)d_out) & 15) == 0) &&
                         (((uintptr_t)d_in[2] & 3) == 0);
    const long long tiles = aligned ? n / kTileElems : 0;
    if (tiles > 0) {
        rezero_main<<<dim3((unsigned)tiles), dim3(kBlock), 0, stream>>>(d_in[0], d_in[1], d_in[2], d_out);
    }
    const long long done = tiles * kTileElems;
    if (done < n) {
        rezero_tail<<<dim3((unsigned)((n - done + 255) / 256)), dim3(256), 0, stream>>>(
            (const unsigned short*)d_in[0], (const unsigned short*)d_in[1], (const unsigned short*)d_in[2],
            (unsigned short*)d_out, done, n);
    }
}
```
